```python
import torch

def _softmax(x, axis=-1):
    return torch.softmax(x, dim=axis)
def _gelu(x):
    return torch.nn.functional.gelu(x, approximate="tanh")
def _silu(x):
    return torch.nn.functional.silu(x)
def _sigmoid(x):
    return torch.sigmoid(x)
def _layernorm(x, axis=-1, eps=1e-5):
    m = x.mean(dim=axis, keepdim=True)
    v = x.var(dim=axis, keepdim=True, unbiased=False)
    return (x - m) / torch.sqrt(v + eps)
def _scatter_add(tgt, idx, src, axis=0):
    return tgt.clone().index_add_(axis, idx, src)
def _segment_sum(src, seg, n):
    out = torch.zeros((n,) + src.shape[1:], dtype=src.dtype)
    return out.index_add_(0, seg, src)
def _pad(x, pw):
    flat = tuple(v for pair in reversed(pw) for v in pair)
    return torch.nn.functional.pad(x, flat)

kernel_name = 'concat_heads__ssm_conv_h3__softmax_full_mha__prompt'

def setup_inputs():

    _w = (lambda a: (lambda t: t.to(torch.bfloat16) if t.is_floating_point() else t)(torch.as_tensor(a)))
    def _A_setup():
        B, S, D = 1, 2048, 512
        ks = 4
        return {
            "q": _w((lambda *s: torch.randn(*s, dtype=torch.bfloat16) * 0.05)(B, S, D)),
            "k": _w((lambda *s: torch.randn(*s, dtype=torch.bfloat16) * 0.4)(B, S, D)),
            "v": _w((lambda *s: torch.randn(*s, dtype=torch.bfloat16) * 0.4)(B, S, D)),
            "shift_k": _w((lambda *s: torch.randn(*s, dtype=torch.bfloat16) * 0.4)(D, ks) * 0.5),
            "ssm_k": _w((lambda *s: torch.randn(*s, dtype=torch.bfloat16) * 1.6)(D, S) * (1.0 / S ** 0.5)),
            "ssm_D": _w((lambda *s: torch.randn(*s, dtype=torch.bfloat16) * 0.4)(D)),
        }
    _A_in = _A_setup()

    def _B_setup():
        B, S, D = 1, 2048, 512
        return {
            "x": _w((lambda *s: torch.randn(*s, dtype=torch.bfloat16) * 0.2)(B, S, D)),
            "wqkv": _w((lambda *s: torch.randn(*s, dtype=torch.bfloat16) * 0.2)(D, 3 * D)),
            "wo": _w((lambda *s: torch.randn(*s, dtype=torch.bfloat16) * 0.05)(D, D)),
        }
    _B_in = _B_setup()

    return {
        "A_q": _A_in["q"],
        "A_k": _A_in["k"],
        "A_v": _A_in["v"],
        "A_shift_k": _A_in["shift_k"],
        "A_ssm_k": _A_in["ssm_k"],
        "A_ssm_D": _A_in["ssm_D"],
        "B_x": _B_in["x"],
        "B_wqkv": _B_in["wqkv"],
        "B_wo": _B_in["wo"],
    }

def reference(A_q, A_k, A_v, A_shift_k, A_ssm_k, A_ssm_D, B_x, B_wqkv, B_wo):
    def _A_ref(q, k, v, shift_k, ssm_k, ssm_D):
        B, S, D = q.shape
        ks = shift_k.shape[-1]
        k_pad = _pad(k, ((0, 0), (ks - 1, 0), (0, 0)))
        k_shift = torch.zeros_like(k)
        for t in range(ks):
            k_shift = k_shift + k_pad[:, t:t + S, :] * shift_k[:, ks - 1 - t][None, None, :]
        kv = k_shift * v
        L = 2 * S
        kv_pad = _pad(kv, ((0, 0), (0, S), (0, 0)))
        kf_pad = _pad(ssm_k, ((0, 0), (0, S)))
        kv_f = (lambda x, axes=(-2,-1): torch.fft.rfft2(x.float(), dim=axes))(kv_pad[:, None, :, :], axes=(1, 2))
        kf_f = (lambda x, axes=(-2,-1): torch.fft.rfft2(x.float(), dim=axes))(kf_pad[:, None, :], axes=(1, 2))
        kf_f = (lambda x, *d: x.permute(*d))(kf_f, 1, 2, 0)
        y_f = kv_f * kf_f[None, :, :, :]
        y = (lambda x, s, axes=(-2,-1): torch.fft.irfft2(x, s=s, dim=axes).to(torch.bfloat16))(y_f, (1, L), axes=(1, 2))[:, 0, :S, :]
        y = y + kv * ssm_D[None, None, :]
        return q * y

    def _B_ref(x, wqkv, wo):
        B, S, D = x.shape
        H = 32; Dh = D // H
        qkv = (lambda x, *d: x.permute(*d))((x @ wqkv).reshape(B, S, 3, H, Dh), 2, 0, 3, 1, 4)
        q, k, v = qkv[0], qkv[1], qkv[2]
        scale = Dh ** -0.5
        scores = torch.einsum("bhqd,bhkd->bhqk", q, k) * scale
        p = _softmax(scores, axis=-1)
        o = torch.einsum("bhqk,bhkd->bhqd", p, v)
        return (lambda x, *d: x.permute(*d))(o, 0, 2, 1, 3).reshape(B, S, D) @ wo

    a = _A_ref(A_q, A_k, A_v, A_shift_k, A_ssm_k, A_ssm_D)
    b = _B_ref(B_x, B_wqkv, B_wo)
    return torch.concatenate([a, b], axis=-1)
```

```cpp
#include <hip/hip_runtime.h>
#include <hip/hip_bf16.h>
#include <stdint.h>

typedef float f32x16 __attribute__((ext_vector_type(16)));
typedef float f32x4 __attribute__((ext_vector_type(4)));
typedef float f32x2 __attribute__((ext_vector_type(2)));
typedef __bf16 bf16x8 __attribute__((ext_vector_type(8)));
typedef __bf16 bf16x2 __attribute__((ext_vector_type(2)));
typedef short s16x4 __attribute__((ext_vector_type(4)));
typedef unsigned u32x4 __attribute__((ext_vector_type(4)));
typedef unsigned u32x2 __attribute__((ext_vector_type(2)));
typedef unsigned long long u64;
#define LAS __attribute__((address_space(3)))
#define GAS __attribute__((address_space(1)))
#define DI __device__ __forceinline__
#define MFMA32(a, b, c) __builtin_amdgcn_mfma_f32_32x32x16_bf16(a, b, c, 0, 0, 0)
#define MFMA16(a, b, c) __builtin_amdgcn_mfma_f32_16x16x32_bf16(a, b, c, 0, 0, 0)
#define RLX_AGENT __ATOMIC_RELAXED, __HIP_MEMORY_SCOPE_AGENT

#define WARM_CODE 3
#define WARM_BYTES 22912
#define POLL_SLEEP 2
#define RETRY_SLEEP 1
#define TAIL_SLEEP 14
#define TAIL_SLEEP2 2
#define FLAGA_N2 1
#define FLAGB_NU 2
#define OUT_AUX 2
constexpr int SEQ = 2048, DM = 512;
constexpr unsigned WS_EPOCH = 0, WS_FLAGS = 4096, FLAG_STRIDE = 128;
constexpr unsigned WS_KVT = 1u << 20;
constexpr unsigned WS_QF = WS_KVT + (2u << 20);
constexpr unsigned WS_KF = WS_QF + (2u << 20);
constexpr unsigned WS_VF = WS_KF + (2u << 20);
constexpr unsigned WS_YT = WS_VF + (2u << 20);
constexpr unsigned WS_OF = WS_YT + (2u << 20);
constexpr unsigned WS_WOF = WS_OF + (2u << 20);
constexpr unsigned WS_USED = WS_WOF + (1u << 20);
constexpr int AUX_SC1 = 16;
constexpr unsigned SPIN_CAP = 1u << 15;
constexpr unsigned SENT = 0xFFFFFFFFu;

struct Params {
  const uint16_t *aq, *ak, *av, *shiftk, *ssmk, *ssmd, *x, *wqkv, *wo;
  uint16_t* out;
  char* ws;
};

constexpr int LDS_BYTES = 160 * 1024;
constexpr int VBLK = 544;
constexpr int LA_V = 0, LA_K = 130 * VBLK, LA_END = LA_K + 66 * 1024;
constexpr int L1_SLOT = 32768, L1_XO = 24576;
constexpr int LC_CS = 4160;
constexpr int LC_H = LA_K + 32768, LC_HCH = 4 * LC_CS;
constexpr int L1_TK = LC_H + 2 * LC_HCH;
constexpr int L1_TKW = 19 * 128 + 512;
constexpr int L1_TT = L1_TK + 4 * L1_TKW;
constexpr int LC_U = LA_END, LC_UCH = 5248;
constexpr int LA_Q = LC_U + 2 * LC_UCH;
constexpr int LE_CNT = LA_Q + 8192;
constexpr int LE_Y = LE_CNT + 512;
static_assert(LC_H >= 3 * L1_SLOT && LC_H + 2 * LC_HCH <= LA_END && (LC_H & 15) == 0 && (L1_TK & 15) == 0 && (L1_TT & 15) == 0 && L1_TT + 8192 <= LE_CNT && LE_Y + 4 * 1152 <= LDS_BYTES, "lds");

DI unsigned cvtpk(float lo, float hi) { f32x2 v = {lo, hi}; bf16x2 b = __builtin_convertvector(v, bf16x2); return __builtin_bit_cast(unsigned, b); }
DI float bflo(unsigned u) { return __uint_as_float(u << 16); }
DI float bfhi(unsigned u) { return __uint_as_float(u & 0xffff0000u); }
DI void glds16(const void* g, unsigned lds_dst) {
  unsigned keep;
  asm volatile("s_mov_b32 %0, m0\n\ts_mov_b32 m0, %2\n\ts_nop 0\n\tglobal_load_lds_dwordx4 %1, off\n\ts_mov_b32 m0, %0" : "=&s"(keep) : "v"(g), "s"(lds_dst) : "memory");
}
DI s16x4 trread(const LAS char* p) { return __builtin_amdgcn_ds_read_tr16_b64_v4i16((LAS s16x4*)p); }
DI bf16x8 cat8(s16x4 a, s16x4 b) { typedef short s16x8 __attribute__((ext_vector_type(8))); s16x8 r = {a[0], a[1], a[2], a[3], b[0], b[1], b[2], b[3]}; return __builtin_bit_cast(bf16x8, r); }
#define RAW_BAR() asm volatile("s_waitcnt lgkmcnt(0)\n\ts_barrier" ::: "memory")
#define ORDER_BEHIND_POLL() __builtin_amdgcn_fence(__ATOMIC_ACQUIRE, "wavefront")
typedef __amdgpu_buffer_rsrc_t rsrc_t;
DI u32x4 ld_sc1(rsrc_t rs, unsigned off) { return __builtin_amdgcn_raw_buffer_load_b128(rs, off, 0, AUX_SC1); }
DI void st_sc1(rsrc_t rs, unsigned off, u32x4 v) { __builtin_amdgcn_raw_buffer_store_b128(v, rs, off, 0, AUX_SC1); }
DI GAS u64* flag_ptr(const Params& P, int set, int b) { return (GAS u64*)(P.ws + WS_FLAGS + (unsigned)(set * 256 + b) * FLAG_STRIDE); }
DI void poll_flags(GAS u64* f, u64 want) {
  for (unsigned spins = 0; spins < SPIN_CAP; ++spins) {
    const u64 v = __hip_atomic_load(f, RLX_AGENT);
    if (__all(v == want)) break;
    __builtin_amdgcn_s_sleep(2);
  }
  ORDER_BEHIND_POLL();
}

#define ATTN_LOOP_ASM \
  "1:\n\t" \
  "s_waitcnt lgkmcnt(2)\n\t" \
  "v_exp_f32 v32, v32\n\t" \
  "v_mfma_f32_32x32x16_bf16 v[0:15], v[102:105], v[84:87], v[0:15]\n\t" \
  "v_exp_f32 v33, v33\n\t" \
  "v_exp_f32 v34, v34\n\t" \
  "ds_read_b128 v[94:97], v88 offset:2048\n\t" \
  "v_exp_f32 v35, v35\n\t" \
  "v_exp_f32 v36, v36\n\t" \
  "ds_read_b128 v[98:101], v89 offset:1088\n\t" \
  "v_exp_f32 v37, v37\n\t" \
  "v_mfma_f32_32x32x16_bf16 v[48:63], v[68:71], v[64:67], v[16:31]\n\t" \
  "v_exp_f32 v38, v38\n\t" \
  "v_exp_f32 v39, v39\n\t" \
  "ds_read_b128 v[102:105], v89 offset:1632\n\t" \
  "v_cvt_pk_bf16_f32 v80, v32, v33\n\t" \
  "v_cvt_pk_bf16_f32 v81, v34, v35\n\t" \
  "v_cvt_pk_bf16_f32 v82, v36, v37\n\t" \
  "v_cvt_pk_bf16_f32 v83, v38, v39\n\t" \
  "v_exp_f32 v40, v40\n\t" \
  "v_exp_f32 v41, v41\n\t" \
  "v_exp_f32 v42, v42\n\t" \
  "v_mfma_f32_32x32x16_bf16 v[0:15], v[72:75], v[80:83], v[0:15]\n\t" \
  "v_exp_f32 v43, v43\n\t" \
  "v_exp_f32 v44, v44\n\t" \
  "v_exp_f32 v45, v45\n\t" \
  "v_exp_f32 v46, v46\n\t" \
  "v_exp_f32 v47, v47\n\t" \
  "v_cvt_pk_bf16_f32 v84, v40, v41\n\t" \
  "v_cvt_pk_bf16_f32 v85, v42, v43\n\t" \
  "v_cvt_pk_bf16_f32 v86, v44, v45\n\t" \
  "v_cvt_pk_bf16_f32 v87, v46, v47\n\t" \
  "s_waitcnt lgkmcnt(0)\n\t" \
  "v_exp_f32 v48, v48\n\t" \
  "v_mfma_f32_32x32x16_bf16 v[0:15], v[76:79], v[84:87], v[0:15]\n\t" \
  "v_exp_f32 v49, v49\n\t" \
  "v_exp_f32 v50, v50\n\t" \
  "ds_read_b128 v[68:71], v88 offset:3072\n\t" \
  "v_exp_f32 v51, v51\n\t" \
  "v_exp_f32 v52, v52\n\t" \
  "ds_read_b128 v[72:75], v89 offset:2176\n\t" \
  "v_exp_f32 v53, v53\n\t" \
  "v_mfma_f32_32x32x16_bf16 v[32:47], v[94:97], v[64:67], v[16:31]\n\t" \
  "v_exp_f32 v54, v54\n\t" \
  "v_exp_f32 v55, v55\n\t" \
  "ds_read_b128 v[76:79], v89 offset:2720\n\t" \
  "v_cvt_pk_bf16_f32 v80, v48, v49\n\t" \
  "v_cvt_pk_bf16_f32 v81, v50, v51\n\t" \
  "v_cvt_pk_bf16_f32 v82, v52, v53\n\t" \
  "v_cvt_pk_bf16_f32 v83, v54, v55\n\t" \
  "v_readfirstlane_b32 s21, v93\n\t" \
  "s_nop 1\n\t" \
  "s_cmp_le_u32 s27, s21\n\t" \
  "s_cbranch_scc1 2f\n\t" \
  "s_setprio 0\n\t" \
  "s_branch 3f\n\t" \
  "2:\n\t" \
  "s_setprio 1\n\t" \
  "3:\n\t" \
  "v_exp_f32 v56, v56\n\t" \
  "v_exp_f32 v57, v57\n\t" \
  "v_exp_f32 v58, v58\n\t" \
  "v_mfma_f32_32x32x16_bf16 v[0:15], v[98:101], v[80:83], v[0:15]\n\t" \
  "v_exp_f32 v59, v59\n\t" \
  "v_exp_f32 v60, v60\n\t" \
  "v_exp_f32 v61, v61\n\t" \
  "v_exp_f32 v62, v62\n\t" \
  "v_exp_f32 v63, v63\n\t" \
  "v_cvt_pk_bf16_f32 v84, v56, v57\n\t" \
  "v_cvt_pk_bf16_f32 v85, v58, v59\n\t" \
  "v_cvt_pk_bf16_f32 v86, v60, v61\n\t" \
  "v_cvt_pk_bf16_f32 v87, v62, v63\n\t" \
  "s_waitcnt lgkmcnt(0)\n\t" \
  "v_exp_f32 v32, v32\n\t" \
  "v_mfma_f32_32x32x16_bf16 v[0:15], v[102:105], v[84:87], v[0:15]\n\t" \
  "v_exp_f32 v33, v33\n\t" \
  "v_exp_f32 v34, v34\n\t" \
  "ds_read_b128 v[94:97], v88 offset:4096\n\t" \
  "v_exp_f32 v35, v35\n\t" \
  "v_exp_f32 v36, v36\n\t" \
  "ds_read_b128 v[98:101], v89 offset:3264\n\t" \
  "v_exp_f32 v37, v37\n\t" \
  "v_mfma_f32_32x32x16_bf16 v[48:63], v[68:71], v[64:67], v[16:31]\n\t" \
  "v_exp_f32 v38, v38\n\t" \
  "v_exp_f32 v39, v39\n\t" \
  "ds_read_b128 v[102:105], v89 offset:3808\n\t" \
  "v_cvt_pk_bf16_f32 v80, v32, v33\n\t" \
  "v_cvt_pk_bf16_f32 v81, v34, v35\n\t" \
  "v_cvt_pk_bf16_f32 v82, v36, v37\n\t" \
  "v_cvt_pk_bf16_f32 v83, v38, v39\n\t" \
  "v_exp_f32 v40, v40\n\t" \
  "v_exp_f32 v41, v41\n\t" \
  "v_exp_f32 v42, v42\n\t" \
  "v_mfma_f32_32x32x16_bf16 v[0:15], v[72:75], v[80:83], v[0:15]\n\t" \
  "v_exp_f32 v43, v43\n\t" \
  "v_exp_f32 v44, v44\n\t" \
  "v_exp_f32 v45, v45\n\t" \
  "v_exp_f32 v46, v46\n\t" \
  "v_exp_f32 v47, v47\n\t" \
  "v_cvt_pk_bf16_f32 v84, v40, v41\n\t" \
  "v_cvt_pk_bf16_f32 v85, v42, v43\n\t" \
  "v_cvt_pk_bf16_f32 v86, v44, v45\n\t" \
  "v_cvt_pk_bf16_f32 v87, v46, v47\n\t" \
  "s_waitcnt lgkmcnt(0)\n\t" \
  "v_exp_f32 v48, v48\n\t" \
  "v_mfma_f32_32x32x16_bf16 v[0:15], v[76:79], v[84:87], v[0:15]\n\t" \
  "v_exp_f32 v49, v49\n\t" \
  "v_exp_f32 v50, v50\n\t" \
  "ds_read_b128 v[68:71], v88 offset:5120\n\t" \
  "v_exp_f32 v51, v51\n\t" \
  "v_exp_f32 v52, v52\n\t" \
  "ds_read_b128 v[72:75], v89 offset:4352\n\t" \
  "v_exp_f32 v53, v53\n\t" \
  "v_mfma_f32_32x32x16_bf16 v[32:47], v[94:97], v[64:67], v[16:31]\n\t" \
  "v_exp_f32 v54, v54\n\t" \
  "v_exp_f32 v55, v55\n\t" \
  "ds_read_b128 v[76:79], v89 offset:4896\n\t" \
  "v_cvt_pk_bf16_f32 v80, v48, v49\n\t" \
  "v_cvt_pk_bf16_f32 v81, v50, v51\n\t" \
  "v_cvt_pk_bf16_f32 v82, v52, v53\n\t" \
  "v_cvt_pk_bf16_f32 v83, v54, v55\n\t" \
  "v_exp_f32 v56, v56\n\t" \
  "v_exp_f32 v57, v57\n\t" \
  "v_exp_f32 v58, v58\n\t" \
  "v_mfma_f32_32x32x16_bf16 v[0:15], v[98:101], v[80:83], v[0:15]\n\t" \
  "v_exp_f32 v59, v59\n\t" \
  "v_exp_f32 v60, v60\n\t" \
  "v_exp_f32 v61, v61\n\t" \
  "v_exp_f32 v62, v62\n\t" \
  "v_exp_f32 v63, v63\n\t" \
  "v_cvt_pk_bf16_f32 v84, v56, v57\n\t" \
  "v_cvt_pk_bf16_f32 v85, v58, v59\n\t" \
  "v_cvt_pk_bf16_f32 v86, v60, v61\n\t" \
  "v_cvt_pk_bf16_f32 v87, v62, v63\n\t" \
  "s_waitcnt lgkmcnt(0)\n\t" \
  "v_exp_f32 v32, v32\n\t" \
  "v_mfma_f32_32x32x16_bf16 v[0:15], v[102:105], v[84:87], v[0:15]\n\t" \
  "v_exp_f32 v33, v33\n\t" \
  "v_exp_f32 v34, v34\n\t" \
  "ds_read_b128 v[94:97], v88 offset:6144\n\t" \
  "v_exp_f32 v35, v35\n\t" \
  "v_exp_f32 v36, v36\n\t" \
  "ds_read_b128 v[98:101], v89 offset:5440\n\t" \
  "v_exp_f32 v37, v37\n\t" \
  "v_mfma_f32_32x32x16_bf16 v[48:63], v[68:71], v[64:67], v[16:31]\n\t" \
  "v_exp_f32 v38, v38\n\t" \
  "v_exp_f32 v39, v39\n\t" \
  "ds_read_b128 v[102:105], v89 offset:5984\n\t" \
  "v_cvt_pk_bf16_f32 v80, v32, v33\n\t" \
  "v_cvt_pk_bf16_f32 v81, v34, v35\n\t" \
  "v_cvt_pk_bf16_f32 v82, v36, v37\n\t" \
  "v_cvt_pk_bf16_f32 v83, v38, v39\n\t" \
  "v_exp_f32 v40, v40\n\t" \
  "v_exp_f32 v41, v41\n\t" \
  "v_exp_f32 v42, v42\n\t" \
  "v_mfma_f32_32x32x16_bf16 v[0:15], v[72:75], v[80:83], v[0:15]\n\t" \
  "v_exp_f32 v43, v43\n\t" \
  "v_exp_f32 v44, v44\n\t" \
  "v_exp_f32 v45, v45\n\t" \
  "v_exp_f32 v46, v46\n\t" \
  "v_exp_f32 v47, v47\n\t" \
  "v_cvt_pk_bf16_f32 v84, v40, v41\n\t" \
  "v_cvt_pk_bf16_f32 v85, v42, v43\n\t" \
  "v_cvt_pk_bf16_f32 v86, v44, v45\n\t" \
  "v_cvt_pk_bf16_f32 v87, v46, v47\n\t" \
  "s_waitcnt lgkmcnt(0)\n\t" \
  "v_exp_f32 v48, v48\n\t" \
  "v_mfma_f32_32x32x16_bf16 v[0:15], v[76:79], v[84:87], v[0:15]\n\t" \
  "v_exp_f32 v49, v49\n\t" \
  "v_exp_f32 v50, v50\n\t" \
  "ds_read_b128 v[68:71], v88 offset:7168\n\t" \
  "v_exp_f32 v51, v51\n\t" \
  "v_exp_f32 v52, v52\n\t" \
  "ds_read_b128 v[72:75], v89 offset:6528\n\t" \
  "v_exp_f32 v53, v53\n\t" \
  "v_mfma_f32_32x32x16_bf16 v[32:47], v[94:97], v[64:67], v[16:31]\n\t" \
  "v_exp_f32 v54, v54\n\t" \
  "v_exp_f32 v55, v55\n\t" \
  "ds_read_b128 v[76:79], v89 offset:7072\n\t" \
  "v_cvt_pk_bf16_f32 v80, v48, v49\n\t" \
  "v_cvt_pk_bf16_f32 v81, v50, v51\n\t" \
  "v_cvt_pk_bf16_f32 v82, v52, v53\n\t" \
  "v_cvt_pk_bf16_f32 v83, v54, v55\n\t" \
  "v_exp_f32 v56, v56\n\t" \
  "v_exp_f32 v57, v57\n\t" \
  "v_exp_f32 v58, v58\n\t" \
  "v_mfma_f32_32x32x16_bf16 v[0:15], v[98:101], v[80:83], v[0:15]\n\t" \
  "v_exp_f32 v59, v59\n\t" \
  "v_exp_f32 v60, v60\n\t" \
  "v_exp_f32 v61, v61\n\t" \
  "v_exp_f32 v62, v62\n\t" \
  "v_exp_f32 v63, v63\n\t" \
  "v_cvt_pk_bf16_f32 v84, v56, v57\n\t" \
  "v_cvt_pk_bf16_f32 v85, v58, v59\n\t" \
  "v_cvt_pk_bf16_f32 v86, v60, v61\n\t" \
  "v_cvt_pk_bf16_f32 v87, v62, v63\n\t" \
  "s_waitcnt lgkmcnt(0)\n\t" \
  "v_exp_f32 v32, v32\n\t" \
  "v_mfma_f32_32x32x16_bf16 v[0:15], v[102:105], v[84:87], v[0:15]\n\t" \
  "v_exp_f32 v33, v33\n\t" \
  "v_exp_f32 v34, v34\n\t" \
  "ds_read_b128 v[94:97], v88 offset:8192\n\t" \
  "v_exp_f32 v35, v35\n\t" \
  "v_exp_f32 v36, v36\n\t" \
  "ds_read_b128 v[98:101], v89 offset:7616\n\t" \
  "v_exp_f32 v37, v37\n\t" \
  "v_mfma_f32_32x32x16_bf16 v[48:63], v[68:71], v[64:67], v[16:31]\n\t" \
  "v_exp_f32 v38, v38\n\t" \
  "v_exp_f32 v39, v39\n\t" \
  "ds_read_b128 v[102:105], v89 offset:8160\n\t" \
  "v_cvt_pk_bf16_f32 v80, v32, v33\n\t" \
  "v_cvt_pk_bf16_f32 v81, v34, v35\n\t" \
  "v_cvt_pk_bf16_f32 v82, v36, v37\n\t" \
  "v_cvt_pk_bf16_f32 v83, v38, v39\n\t" \
  "v_exp_f32 v40, v40\n\t" \
  "v_exp_f32 v41, v41\n\t" \
  "v_exp_f32 v42, v42\n\t" \
  "v_mfma_f32_32x32x16_bf16 v[0:15], v[72:75], v[80:83], v[0:15]\n\t" \
  "v_exp_f32 v43, v43\n\t" \
  "v_exp_f32 v44, v44\n\t" \
  "v_exp_f32 v45, v45\n\t" \
  "v_exp_f32 v46, v46\n\t" \
  "v_exp_f32 v47, v47\n\t" \
  "v_cvt_pk_bf16_f32 v84, v40, v41\n\t" \
  "v_cvt_pk_bf16_f32 v85, v42, v43\n\t" \
  "v_cvt_pk_bf16_f32 v86, v44, v45\n\t" \
  "v_cvt_pk_bf16_f32 v87, v46, v47\n\t" \
  "s_waitcnt lgkmcnt(0)\n\t" \
  "v_exp_f32 v48, v48\n\t" \
  "v_mfma_f32_32x32x16_bf16 v[0:15], v[76:79], v[84:87], v[0:15]\n\t" \
  "v_exp_f32 v49, v49\n\t" \
  "v_exp_f32 v50, v50\n\t" \
  "ds_read_b128 v[68:71], v88 offset:9216\n\t" \
  "v_exp_f32 v51, v51\n\t" \
  "v_exp_f32 v52, v52\n\t" \
  "ds_read_b128 v[72:75], v89 offset:8704\n\t" \
  "v_exp_f32 v53, v53\n\t" \
  "v_mfma_f32_32x32x16_bf16 v[32:47], v[94:97], v[64:67], v[16:31]\n\t" \
  "v_exp_f32 v54, v54\n\t" \
  "v_exp_f32 v55, v55\n\t" \
  "ds_read_b128 v[76:79], v89 offset:9248\n\t" \
  "v_cvt_pk_bf16_f32 v80, v48, v49\n\t" \
  "v_cvt_pk_bf16_f32 v81, v50, v51\n\t" \
  "v_cvt_pk_bf16_f32 v82, v52, v53\n\t" \
  "v_cvt_pk_bf16_f32 v83, v54, v55\n\t" \
  "v_exp_f32 v56, v56\n\t" \
  "v_exp_f32 v57, v57\n\t" \
  "v_exp_f32 v58, v58\n\t" \
  "v_mfma_f32_32x32x16_bf16 v[0:15], v[98:101], v[80:83], v[0:15]\n\t" \
  "v_exp_f32 v59, v59\n\t" \
  "v_exp_f32 v60, v60\n\t" \
  "v_exp_f32 v61, v61\n\t" \
  "v_exp_f32 v62, v62\n\t" \
  "v_exp_f32 v63, v63\n\t" \
  "v_add_u32 v88, 8192, v88\n\t" \
  "v_add_u32 v89, 8704, v89\n\t" \
  "s_add_u32 s27, s27, 1\n\t" \
  "v_mov_b32 v92, s27\n\t" \
  "ds_write_b32 v90, v92\n\t" \
  "ds_read_b32 v93, v91\n\t" \
  "s_sub_u32 s26, s26, 1\n\t" \
  "s_cmp_lg_u32 s26, 0\n\t" \
  "v_cvt_pk_bf16_f32 v84, v56, v57\n\t" \
  "v_cvt_pk_bf16_f32 v85, v58, v59\n\t" \
  "v_cvt_pk_bf16_f32 v86, v60, v61\n\t" \
  "v_cvt_pk_bf16_f32 v87, v62, v63\n\t" \
  "s_cbranch_scc1 1b\n\t" \
  ""
#define ATTN_LOOP_UNR 8
#define ATTN_LOOP_BAL 1

DI f32x2 unpk(unsigned u) { return (f32x2){bflo(u), bfhi(u)}; }
DI unsigned pk2(f32x2 v) { return cvtpk(v[0], v[1]); }
DI unsigned kv_pair(f32x2 k3, f32x2 k2, f32x2 k1, f32x2 k0, f32x2 s0, f32x2 s1, f32x2 s2, f32x2 s3, unsigned v) {
  const unsigned p0 = pk2(k3 * s3), p1 = pk2(k2 * s2), p2 = pk2(k1 * s1), p3 = pk2(k0 * s0);
  unsigned a = pk2(unpk(p0) + unpk(p1));
  a = pk2(unpk(a) + unpk(p2));
  a = pk2(unpk(a) + unpk(p3));
  return pk2(unpk(a) * unpk(v));
}
DI void gate(GAS u64* f, u64 want, unsigned mask, LAS unsigned* word, unsigned tag, bool poller) {
  if (poller) {
    if (__hip_atomic_load(word, __ATOMIC_RELAXED, __HIP_MEMORY_SCOPE_WORKGROUP) < tag) {
      unsigned m = 0;
      for (unsigned spins = 0; spins < SPIN_CAP; ++spins) {
        m = (unsigned)__ballot(__hip_atomic_load(f, RLX_AGENT) == want);
        if ((m & mask) == mask) break;
        __builtin_amdgcn_s_sleep(POLL_SLEEP);
      }
      if ((threadIdx.x & 63) == 0) __hip_atomic_store(word, tag, __ATOMIC_RELAXED, __HIP_MEMORY_SCOPE_WORKGROUP);
    }
  } else {
    while (__hip_atomic_load(word, __ATOMIC_RELAXED, __HIP_MEMORY_SCOPE_WORKGROUP) < tag) __builtin_amdgcn_s_sleep(1);
  }
  ORDER_BEHIND_POLL();
}
DI unsigned max8(u32x4 a, u32x4 b) { return max(max(max(a[0], a[1]), max(a[2], a[3])), max(max(b[0], b[1]), max(b[2], b[3]))); }

struct ConvFr { bf16x8 A[8], F[8]; };
DI void conv_ld(ConvFr& f, unsigned ua, unsigned uf) {
#pragma unroll
  for (int al = 0; al < 8; ++al) f.F[al] = *(const LAS bf16x8*)(uintptr_t)(uf + (7 - al) * 512);
#pragma unroll
  for (int al = 0; al < 8; ++al) {
    const u32x2 lo = *(const volatile LAS u32x2*)(uintptr_t)(ua + al * 512), hi = *(const volatile LAS u32x2*)(uintptr_t)(ua + al * 512 + 8);
    f.A[al] = __builtin_bit_cast(bf16x8, (u32x4){lo[0], lo[1], hi[0], hi[1]});
  }
}
DI void conv_mul(const ConvFr& f, f32x4 (&acc)[4]) {
#pragma unroll
  for (int al = 0; al < 8; ++al)
#pragma unroll
    for (int q = 0; q < 4; ++q) if (al <= 7 - 2 * q) acc[q] = MFMA16(f.A[al], f.F[7 - 2 * q - al], acc[q]);
}

#define SBAR() __builtin_amdgcn_sched_barrier(0)
DI void conv_wave(const Params& P, LAS char* lds, u64 epoch, unsigned ua, unsigned uf, f32x4 (&acc)[4]) {
  ConvFr fa, fb;
  asm volatile("" : "+v"(ua), "+v"(uf));
  conv_ld(fa, ua, uf); SBAR();
#pragma unroll 1
  for (int nu = 0; nu < 8; nu += 2) {
    if (nu == FLAGB_NU) {
      asm volatile("s_waitcnt vmcnt(0)" ::: "memory");
      if ((threadIdx.x & 63) == 0) {
        const unsigned old = __hip_atomic_fetch_add((LAS unsigned*)(lds + LE_CNT + 4), 1u, __ATOMIC_RELAXED, __HIP_MEMORY_SCOPE_WORKGROUP);
        if (old == 3) __hip_atomic_store(flag_ptr(P, 1, blockIdx.x), epoch + 1, RLX_AGENT);
      }
    }
    unsigned ub = ua + 64, ug = uf + 64;
    asm volatile("" : "+v"(ub), "+v"(ug));
    conv_ld(fb, ub, ug); conv_mul(fa, acc);
#pragma unroll
    for (int q = 0; q < 20; ++q) { __builtin_amdgcn_sched_group_barrier(0x8, 1, 0); __builtin_amdgcn_sched_group_barrier(0x100, 2, 0); }
    SBAR();
    ua += 128; uf += 128;
    asm volatile("" : "+v"(ua), "+v"(uf));
    conv_ld(fa, ua, uf); conv_mul(fb, acc);
#pragma unroll
    for (int q = 0; q < 20; ++q) { __builtin_amdgcn_sched_group_barrier(0x8, 1, 0); __builtin_amdgcn_sched_group_barrier(0x100, 2, 0); }
    SBAR();
  }
}

__global__ __launch_bounds__(512, 2) void k_fused(Params P) {
  __shared__ __attribute__((aligned(16))) char lds_[LDS_BYTES];
  LAS char* lds = (LAS char*)lds_;
  const unsigned ldsb = (unsigned)(uintptr_t)lds;
  const int tid = threadIdx.x, lane = tid & 63; const int w = __builtin_amdgcn_readfirstlane(tid >> 6);
  const int b = blockIdx.x, j = b & 7, i = b >> 3;
  const rsrc_t rs = __builtin_amdgcn_make_buffer_rsrc((void*)P.ws, 0, WS_USED, 0x00020000);
  const rsrc_t ro = __builtin_amdgcn_make_buffer_rsrc((void*)P.out, 0, SEQ * 1024 * 2, 0x00020000);
  const int g = lane >> 4, h2 = lane >> 5;
  const int ah = 4 * j + (i >> 3), ap_ = i & 7, aT = 8 * ap_ + w;
  const int tk = 8 * (j & 3) + (i >> 2), hq = 4 * (j >> 2) + (i & 3), rot = ((i >> 2) + 2 * (i & 3)) & 7;
  const int wl = w & 3;
  const unsigned offQ = WS_QF + ((unsigned)((4 * hq + wl) * 64 + 2 * tk) * 64 + lane) * 16;
  const unsigned offV = WS_VF + ((unsigned)(((4 * hq + (wl & 2) + (g & 1)) * 128 + 2 * (2 * tk + (wl & 1))) * 2 + h2) * 16 + (lane & 15)) * 16;
  const int t0 = 64 * i, chw = 64 * j, chb = 64 * j + 2 * i;
  u64 epoch;
  u32x4 kvk[2];
  if (w == 7) {
    u64 pc; asm volatile("s_getpc_b64 %0" : "=s"(pc));
    const char* base = (const char*)(pc & ~127ull);
#pragma unroll
    for (int e = 0; e < WARM_CODE; ++e) {
      unsigned keep;
      asm volatile("s_mov_b32 %0, m0\n\ts_mov_b32 m0, %2\n\ts_nop 0\n\tglobal_load_lds_dword %1, off\n\ts_mov_b32 m0, %0" : "=&s"(keep) : "v"(base + min(e * 8192 + lane * 128, WARM_BYTES - 128)), "s"(ldsb + LE_CNT + 128) : "memory");
    }
  }
  if (w < 4) {
    const int rp = tid >> 3, p8 = tid & 7;
    u32x4 kk0[2], vv[2];
#pragma unroll
    for (int e = 0; e < 2; ++e) kk0[e] = *(const GAS u32x4*)(P.ak + (size_t)(t0 + 2 * rp + e) * DM + chw + p8 * 8);
    const int hr = t0 + 16 * w - 3 + (lane >> 3);
    const uint16_t* hsrc = lane < 24 ? P.ak + (size_t)max(hr, 0) * DM + chw + p8 * 8 : P.shiftk + chw * 4 + ((lane - 24) & 31) * 8;
    u32x4 halo = *(const GAS u32x4*)hsrc;
#pragma unroll
    for (int e = 0; e < 2; ++e) vv[e] = *(const GAS u32x4*)(P.av + (size_t)(t0 + 2 * rp + e) * DM + chw + p8 * 8);
    epoch = __hip_atomic_load((GAS u64*)(P.ws + WS_EPOCH), RLX_AGENT);
    u32x4 d0[2], e0; u32x2 d1[2], e1; unsigned wlo[2], whi[2];
#pragma unroll
    for (int c2 = 0; c2 < 2; ++c2) {
      const uint16_t* hrow = P.ssmk + (size_t)(chb + c2) * SEQ;
      d0[c2] = *(const GAS u32x4*)(hrow + 8 * max(tid - 2, 0)); d1[c2] = *(const GAS u32x2*)(hrow + 8 * max(tid - 1, 0));
    }
    { const uint16_t* hrow = P.ssmk + (size_t)(chb + (tid & 1)) * SEQ;
      e0 = *(const GAS u32x4*)(hrow + 8 * (254 + ((tid >> 1) & 1))); e1 = *(const GAS u32x2*)(hrow + 8 * 255); }
#pragma unroll
    for (int f = 0; f < 2; ++f) { const int ct = b >> 4, hw = 2 * (b & 15) + f, l2 = tid >> 2, dw = tid & 3;
      const uint16_t* wp = P.wo + (size_t)(16 * hw + 8 * (dw >> 1) + 4 * (l2 >> 5) + 2 * (dw & 1)) * DM + 32 * ct + (l2 & 31);
      wlo[f] = wp[0]; whi[f] = wp[DM]; }
    {
      LAS char* tk_ = lds + L1_TK + w * L1_TKW;
      const int lr = lane >> 3;
#pragma unroll
      for (int e = 0; e < 2; ++e) *(LAS u32x4*)(tk_ + (3 + 2 * lr + e) * 128 + p8 * 16) = kk0[e];
      if (lane < 24 && hr < 0) halo = (u32x4){0, 0, 0, 0};
      if (lane < 56) *(LAS u32x4*)(tk_ + lane * 16 + (lane < 24 ? 0 : 19 * 128 - 24 * 16)) = halo;
      const LAS char* kr = tk_ + 2 * lr * 128 + p8 * 16;
      const u32x4 k3 = *(const LAS u32x4*)(kr), k2 = *(const LAS u32x4*)(kr + 128), k1 = *(const LAS u32x4*)(kr + 256);
#pragma unroll
      for (int q = 0; q < 4; ++q) {
        const u32x4 s = *(const LAS u32x4*)(tk_ + 19 * 128 + p8 * 64 + q * 16);
        const f32x2 s0 = {bflo(s[0]), bflo(s[2])}, s1 = {bfhi(s[0]), bfhi(s[2])}, s2 = {bflo(s[1]), bflo(s[3])}, s3 = {bfhi(s[1]), bfhi(s[3])};
        const f32x2 f3 = unpk(k3[q]), f2 = unpk(k2[q]), f1 = unpk(k1[q]), f0 = unpk(kk0[0][q]), fn = unpk(kk0[1][q]);
        kvk[0][q] = kv_pair(f3, f2, f1, f0, s0, s1, s2, s3, vv[0][q]);
        kvk[1][q] = kv_pair(f2, f1, f0, fn, s0, s1, s2, s3, vv[1][q]);
      }
      const int x = 62 - 2 * rp;
      LAS char* tw = lds + L1_TT + (8 * p8) * 128 + (((x >> 3) ^ p8) << 4) + (x & 7) * 2;
#pragma unroll
      for (int q = 0; q < 4; ++q) {
        *(LAS unsigned*)(tw + (2 * q) * 128) = __builtin_amdgcn_perm(kvk[0][q], kvk[1][q], 0x05040100u);
        *(LAS unsigned*)(tw + (2 * q + 1) * 128) = __builtin_amdgcn_perm(kvk[0][q], kvk[1][q], 0x07060302u);
      }
    }
    RAW_BAR();
    {
#pragma unroll
      for (int e = 0; e < 2; ++e) {
        const int ch = rp + 32 * e;
        st_sc1(rs, WS_KVT + ((unsigned)(chw + ch) * SEQ + (1984 - t0) + 8 * p8) * 2, *(const LAS u32x4*)(lds + L1_TT + ch * 128 + ((p8 ^ (ch >> 3)) << 4)));
      }
    }
    const LAS char* xp = lds + L1_XO + (lane & 31) * 128; const int xsw = ((lane & 31) >> 1) & 7;
    const int tq = (lane & 15) >> 2;
    const int wro = (8 * h2 + tq) * 128 + (lane & 3) * 8;
    const LAS char* wqk = lds + (g & 1) * 8192 + wro + ((wl ^ (tq & 2) ^ (g & 1)) * 32);
    const LAS char* wv = lds + 16384 + wro + ((((wl & 2) + (g & 1)) ^ (tq & 2)) * 32);
    f32x16 aq0 = {}, aq1 = {}, aV = {};
    bf16x8 x0[2][4], x1[2][4], wf[2][4], vf[2][4];
    unsigned uqk = (unsigned)(uintptr_t)wqk, uv = (unsigned)(uintptr_t)wv, ux[4];
#pragma unroll
    for (int ks = 0; ks < 4; ++ks) ux[ks] = (unsigned)(uintptr_t)xp + (((2 * ks + h2) ^ xsw) << 4);
    auto rdfr = [&](int bf, unsigned so) {
      const unsigned a = uqk + so, c = uv + so;
#pragma unroll
      for (int ks = 0; ks < 4; ++ks) {
        const unsigned x = ux[ks] + so;
        wf[bf][ks] = cat8(trread((const LAS char*)(uintptr_t)(a + ks * 2048)), trread((const LAS char*)(uintptr_t)(a + ks * 2048 + 512)));
        x0[bf][ks] = *(const LAS bf16x8*)(uintptr_t)(x);
        x1[bf][ks] = *(const LAS bf16x8*)(uintptr_t)(x + 4096);
        vf[bf][ks] = cat8(trread((const LAS char*)(uintptr_t)(c + ks * 2048)), trread((const LAS char*)(uintptr_t)(c + ks * 2048 + 512)));
      }
    };
    auto mul = [&](int bf) {
#pragma unroll
      for (int ks = 0; ks < 4; ++ks) {
        aq0 = MFMA32(wf[bf][ks], x0[bf][ks], aq0); aq1 = MFMA32(wf[bf][ks], x1[bf][ks], aq1);
        aV = MFMA32((wl & 1) ? x1[bf][ks] : x0[bf][ks], vf[bf][ks], aV);
      }
    };
#define GEMM_STEP(RD, MU) do { rdfr(RD, so); mul(MU); \
      _Pragma("unroll") for (int q = 0; q < 12; ++q) { __builtin_amdgcn_sched_group_barrier(0x8, 1, 0); __builtin_amdgcn_sched_group_barrier(0x100, 4, 0); } \
      asm volatile("" : "+v"(aq0), "+v"(aq1), "+v"(aV)); so = so == 2 * L1_SLOT ? 0 : so + L1_SLOT; } while (0)
    rdfr(0, 0);
    if (tid == 0) { *(LAS u64*)(lds + LE_CNT + 16) = epoch; *(LAS unsigned*)(lds + LE_CNT + 8) = 0; *(LAS unsigned*)(lds + LE_CNT + 24) = 0; }
    RAW_BAR();
    unsigned so = L1_SLOT;
#pragma unroll 1
    for (int n2 = 0; n2 < 3; ++n2) {
      GEMM_STEP(1, 0);
      if (n2 == FLAGA_N2) {
        asm volatile("s_waitcnt vmcnt(0) lgkmcnt(0)\n\ts_barrier" ::: "memory");
        if (tid == 0) __hip_atomic_store(flag_ptr(P, 0, b), epoch + 1, RLX_AGENT);
      } else RAW_BAR();
      GEMM_STEP(0, 1);
      if (n2 == 1) {
      auto put = [&](u32x4 a_, u32x2 c_, int c2, int Yy) {
        unsigned d[5] = {a_[1], a_[2], a_[3], c_[0], c_[1]}, o2[5];
        o2[0] = __builtin_amdgcn_alignbit(a_[1], a_[0], 16);
#pragma unroll
        for (int e = 0; e < 4; ++e) o2[e + 1] = __builtin_amdgcn_alignbit(d[e + 1], d[e], 16);
        LAS char* dst = lds + LC_H + c2 * LC_HCH + Yy * 16;
        *(LAS u32x4*)(dst) = a_; *(LAS u32x4*)(dst + LC_CS) = (u32x4){o2[0], o2[1], o2[2], o2[3]};
        *(LAS u32x4*)(dst + 2 * LC_CS) = (u32x4){d[0], d[1], d[2], d[3]}; *(LAS u32x4*)(dst + 3 * LC_CS) = (u32x4){o2[1], o2[2], o2[3], o2[4]};
      };
      const u32x4 z4 = {0, 0, 0, 0}; const u32x2 z2 = {0, 0};
#pragma unroll
      for (int c2 = 0; c2 < 2; ++c2) put(tid >= 2 ? d0[c2] : z4, tid >= 1 ? d1[c2] : z2, c2, tid);
      if (tid < 4) put(e0, (tid >> 1) ? z2 : e1, tid & 1, 256 + (tid >> 1));

      }
      RAW_BAR();
    }
#pragma unroll
    for (int e = 0; e < 2; ++e) __builtin_amdgcn_raw_buffer_store_b32(wlo[e] | (whi[e] << 16), rs, WS_WOF + b * 2048 + e * 1024 + tid * 4, 0, AUX_SC1);
    GEMM_STEP(1, 0);
    RAW_BAR();
    mul(1);
    {
      const float qs = 0.25f * 1.4426950408889634f;
      auto pk8 = [&](const f32x16& a, int o, float s) { u32x4 r = {cvtpk(a[o] * s, a[o + 1] * s), cvtpk(a[o + 2] * s, a[o + 3] * s), cvtpk(a[o + 4] * s, a[o + 5] * s), cvtpk(a[o + 6] * s, a[o + 7] * s)};
        return r; };
      st_sc1(rs, offQ + (WS_KF - WS_QF), pk8(aq0, 8, 1.f)); st_sc1(rs, offQ + (WS_KF - WS_QF) + 1024, pk8(aq1, 8, 1.f));
      st_sc1(rs, offV, pk8(aV, 0, 1.f)); st_sc1(rs, offV + 512, pk8(aV, 8, 1.f));
      st_sc1(rs, offQ, pk8(aq0, 0, qs)); st_sc1(rs, offQ + 1024, pk8(aq1, 0, qs));
    }
    RAW_BAR();
    {
      const int c2 = w >> 1, par = w & 1;
      const int a = lane & 15, sg = (a + 1) & 3;
      const unsigned ua = ldsb + LC_H + c2 * LC_HCH + sg * LC_CS + ((a + 1 - sg) + 8 * g) * 2;
      const unsigned uf = ldsb + LC_U + c2 * LC_UCH + (2032 - 16 * a + 8 * g - 7 * 256 + (1 - par) * 256) * 2;
      f32x4 acc[4] = {};
      conv_wave(P, lds, epoch, ua, uf, acc);
      asm volatile("s_waitcnt lgkmcnt(0)" ::: "memory");
      if (lane == 0) __hip_atomic_fetch_add((LAS unsigned*)(lds + LE_CNT + 24), 1u, __ATOMIC_RELAXED, __HIP_MEMORY_SCOPE_WORKGROUP);
#pragma unroll
      for (int e = 0; e < 2; ++e) *(LAS u32x4*)(lds + LA_Q + (2 * (tid >> 3) + e) * 128 + (tid & 7) * 16) = kvk[e];
#pragma unroll
      for (int q = 0; q < 4; ++q) {
        const int cb = 6 - 2 * q + par;
        const u32x2 o = {cvtpk(acc[q][0], acc[q][1]), cvtpk(acc[q][2], acc[q][3])};
        __builtin_amdgcn_raw_buffer_store_b64(o, rs, WS_YT + ((unsigned)(chb + c2) * SEQ + 256 * cb + 16 * (lane & 15) + 4 * g) * 2, 0, AUX_SC1);
      }
    }
  } else {
    const int r8 = lane >> 3, p8 = lane & 7, lt = tid & 255;
    const int k0 = 16 * wl + r8;
    const uint16_t* wq_src = P.wqkv + (size_t)k0 * 1536 + 64 * hq + ((((p8 >> 1) ^ (k0 & 2)) << 1) + (p8 & 1)) * 8;
    const uint16_t* wk_src = P.wqkv + (size_t)k0 * 1536 + 512 + 64 * hq + ((((p8 >> 1) ^ (k0 & 2) ^ 1) << 1) + (p8 & 1)) * 8;
    const uint16_t* x_src[2];
#pragma unroll
    for (int e = 0; e < 2; ++e) { const int row = k0 + 8 * e; x_src[e] = P.x + (size_t)(64 * tk + row) * DM + ((p8 ^ ((row >> 1) & 7)) << 3); }
    unsigned dsl = ldsb + wl * 2048;
    int cch = rot;
    auto issue = [&]() {
      const size_t o = (size_t)cch * 64 * 1536;
#pragma unroll
      for (int e = 0; e < 2; ++e) {
        glds16(wq_src + o + e * 8 * 1536, dsl + e * 1024); glds16(wk_src + o + e * 8 * 1536, dsl + 8192 + e * 1024); glds16(wq_src + o + e * 8 * 1536 + 1024, dsl + 16384 + e * 1024);
        glds16(x_src[e] + 64 * cch, dsl + L1_XO + e * 1024);
      }
      cch = (cch + 1) & 7; dsl = dsl >= ldsb + 2 * L1_SLOT ? dsl - 2 * L1_SLOT : dsl + L1_SLOT;
    };
#define WB(N) asm volatile("s_waitcnt vmcnt(" #N ") lgkmcnt(0)\n\ts_barrier" ::: "memory")
#pragma unroll 1
    for (int n = 0; n < 8; ++n) {
      issue();
      if (n == 2) WB(16);
      if (n >= 2) WB(8);
    }
    WB(0);
#undef WB
    epoch = *(const LAS u64*)(lds + LE_CNT + 16);
    gate(flag_ptr(P, 0, j + 8 * (lane & 31)), epoch + 1, 0xFFFFFFFFu, (LAS unsigned*)(lds + LE_CNT + 8), 1u, w == 4);
    u32x4 uu[2];
#pragma unroll
    for (int c2 = 0; c2 < 2; ++c2) uu[c2] = ld_sc1(rs, WS_KVT + ((unsigned)(chb + c2) * SEQ + 8 * lt) * 2);
    asm volatile("s_barrier" ::: "memory");
#pragma unroll
    for (int c2 = 0; c2 < 2; ++c2) *(LAS u32x4*)(lds + LC_U + c2 * LC_UCH + lt * 16) = uu[c2];
    if (lt < 144) *(LAS u32x4*)(lds + LC_U + (lt >= 72 ? LC_UCH + (lt - 72) * 16 : lt * 16) + 4096) = (u32x4){0, 0, 0, 0};
    if (lt == 255) { *(LAS unsigned*)(lds + LE_CNT) = 0; *(LAS unsigned*)(lds + LE_CNT + 4) = 0; }
    if (lt < 8) *(LAS unsigned*)(lds + LE_CNT + 32 + lt * 4) = 0;

    RAW_BAR();
    { const u32x4 sent4 = {SENT, SENT, SENT, SENT};
      st_sc1(rs, WS_OF + ((unsigned)((aT - 4) * 32 + ah) * 64 + lane) * 16, sent4); st_sc1(rs, WS_OF + ((unsigned)(aT * 32 + ah) * 64 + lane) * 16, sent4); }
    if (lt < 256) *(LAS u32x4*)(lds + LA_V + (lt >> 1) * VBLK + (lt & 1) * 272 + 256) = (u32x4){0x3f803f80u, 0x3f803f80u, 0x3f803f80u, 0x3f803f80u};
    if (lt < 4) *(LAS u32x4*)(lds + LA_V + (128 + (lt >> 1)) * VBLK + (lt & 1) * 272 + 256) = (u32x4){0x3f803f80u, 0x3f803f80u, 0x3f803f80u, 0x3f803f80u};
  }
  u32x4 qf;
  {
    gate(flag_ptr(P, 1, ((lane & 31) >> 3) + 4 * (ah >> 4) + 8 * (((ah >> 2) & 3) + 4 * (lane & 7))), epoch + 1, 0xFFFFFFFFu, (LAS unsigned*)(lds + LE_CNT + 8), 2u, w == 4);
    const int hb = 16 * (w >> 2);
    const unsigned psrc = (wl < 2 ? WS_KF + (unsigned)(ah * 64 + wl) * 1024 : WS_VF + (unsigned)(ah * 128 + 2 * (wl - 2)) * 512) + lane * 16 + hb * 2048;
    u32x4 kvs[16];
    qf = ld_sc1(rs, WS_QF + ((unsigned)(ah * 64 + aT) * 64 + lane) * 16);
#pragma unroll
    for (int s = 0; s < 16; ++s) kvs[s] = ld_sc1(rs, psrc + s * 2048);
    if (w >= 4) {
      while (__hip_atomic_load((LAS unsigned*)(lds + LE_CNT + 24), __ATOMIC_RELAXED, __HIP_MEMORY_SCOPE_WORKGROUP) < 4u) __builtin_amdgcn_s_sleep(1);
      asm volatile("" ::: "memory");
    }
    LAS char* pdst = lds + (wl < 2 ? LA_K + wl * 1024 + lane * 16 : LA_V + (2 * (wl - 2) + h2) * VBLK + ((lane >> 4) & 1) * 272 + (lane & 15) * 16);
    const int pstep = wl < 2 ? 2048 : 4 * VBLK;
#pragma unroll
    for (int s = 0; s < 16; ++s) *(LAS u32x4*)(pdst + (hb + s) * pstep) = kvs[s];
    asm volatile("s_waitcnt vmcnt(0)" ::: "memory");
  }
  RAW_BAR();
  if (tid == 0) __hip_atomic_store(flag_ptr(P, 2, b), epoch + 1, RLX_AGENT);
  const int T = 8 * j + (i >> 2), cgp = i & 3;
  bf16x8 wf[32]; u32x4 eq[2], ey[2], edd;
  {
    const int r = lane & 31;
    unsigned kaddr = (unsigned)(uintptr_t)(lds + LA_K + lane * 16);
    unsigned vaddr = (unsigned)(uintptr_t)(lds + LA_V + h2 * 272 + (r < 16 ? r : 16) * 16);
    f32x16 negm, o = {}, sA;
    u32x4 kf, v0, v1, v1b, pb = {0, 0, 0, 0};
    const unsigned pme = ldsb + LE_CNT + 32 + w * 4, ppa = ldsb + LE_CNT + 32 + (w ^ 4) * 4; unsigned pprog = 0;
    {
      const bf16x8 k0 = *(const LAS bf16x8*)(lds + LA_K + lane * 16);
      const f32x16 z = {};
      const f32x16 s = MFMA32(k0, __builtin_bit_cast(bf16x8, qf), z);
      float m = s[0];
#pragma unroll
      for (int e = 1; e < 16; ++e) m = fmaxf(m, s[e]);
      auto rr = __builtin_amdgcn_permlane32_swap(__float_as_uint(m), __float_as_uint(m), false, false);
      m = fmaxf(__uint_as_float(rr[0]), __uint_as_float(rr[1]));
#pragma unroll
      for (int e = 0; e < 16; ++e) { negm[e] = -m; sA[e] = s[e] - m; }
      kf = *(const LAS u32x4*)(lds + LA_K + lane * 16 + 1024);
      v0 = *(const LAS u32x4*)(lds + LA_V + h2 * 272 + (r < 16 ? r : 16) * 16);
      v1 = *(const LAS u32x4*)(lds + LA_V + VBLK + h2 * 272 + (r < 16 ? r : 16) * 16);
      v1b = v0;
    }
    asm volatile("s_branch 9f\n\tattn_tiles_sub:\n\ts_nop 1\n\t" ATTN_LOOP_ASM "s_waitcnt lgkmcnt(0)\n\ts_setpc_b64 s[24:25]\n\t9:" ::: "memory");
#define RUN_TILES(NT_, DONE_) do { constexpr int cnt = (NT_) / ATTN_LOOP_UNR, prog = (DONE_) / ATTN_LOOP_UNR; \
    asm volatile("s_mov_b32 s26, %[cnt]\n\ts_mov_b32 s27, %[prog]\n\ts_getpc_b64 s[24:25]\n\t1:\n\ts_add_u32 s24, s24, 2f-1b\n\ts_addc_u32 s25, s25, 0\n\ts_branch attn_tiles_sub\n\t2:" \
      : "+{v[0:15]}"(o), "+{v[32:47]}"(sA), "+{v[68:71]}"(kf), "+{v[72:75]}"(v0), "+{v[76:79]}"(v1), "+{v[102:105]}"(v1b), "+{v[84:87]}"(pb), "+{v88}"(kaddr), "+{v89}"(vaddr), "+{v93}"(pprog) \
      : "{v[16:31]}"(negm), "{v[64:67]}"(qf), "{v90}"(pme), "{v91}"(ppa), [cnt] "i"(cnt), [prog] "i"(prog) \
      : "v48","v49","v50","v51","v52","v53","v54","v55","v56","v57","v58","v59","v60","v61","v62","v63","v80","v81","v82","v83","v92","v94","v95","v96","v97","v98","v99","v100","v101","s21","s24","s25","s26","s27","scc","memory"); } while (0)
    RUN_TILES(8, 0);
    GAS u64* fT = flag_ptr(P, 2, lane < 32 ? j + 8 * lane : ((lane & 31) >> 2) + 8 * ((lane & 3) * 8 + (T >> 3)));
    GAS u64* fW = flag_ptr(P, 2, 64 * cgp + lane);
    const u64 ft0 = __hip_atomic_load(fT, RLX_AGENT), fw0 = __hip_atomic_load(fW, RLX_AGENT);
    RUN_TILES(40, 8);
    if (!__all(ft0 == epoch + 1)) poll_flags(fT, epoch + 1);
    if (!__all(fw0 == epoch + 1)) poll_flags(fW, epoch + 1);
    ORDER_BEHIND_POLL();
    if (w < 4) {
#pragma unroll
      for (int e = 0; e < 22; ++e) wf[e] = __builtin_bit_cast(bf16x8, ld_sc1(rs, WS_WOF + ((unsigned)((4 * cgp + w) * 32 + e) * 64 + lane) * 16));
    } else {
      const int cg = lane & 7;
      edd = *(const GAS u32x4*)(P.ssmd + 64 * j + 8 * cg);
#pragma unroll
      for (int ps = 0; ps < 2; ++ps) {
        eq[ps] = *(const GAS u32x4*)(P.aq + (size_t)(64 * i + 16 * wl + 2 * (lane >> 3) + ps) * DM + 64 * j + 8 * cg);
        ey[ps] = ld_sc1(rs, WS_YT + ((unsigned)(64 * j + lane) * SEQ + 64 * i + 16 * wl + 8 * ps) * 2);
      }
    }
    RUN_TILES(16, 48);
    __builtin_amdgcn_s_setprio(0);
    asm volatile("s_nop 1\n\tv_mfma_f32_32x32x16_bf16 v[0:15], v[102:105], v[84:87], v[0:15]\n\ts_nop 15\n\ts_nop 3" : "+{v[0:15]}"(o) : "{v[102:105]}"(v1b), "{v[84:87]}"(pb));
    {
      bool bad = !(o[8] < 1.0e36f);
#pragma unroll
      for (int e = 0; e < 8; ++e) bad = bad || !(__builtin_fabsf(o[e]) < 3.0e38f);
      if (__builtin_expect(__any(bad), 0)) {
        float m = -__builtin_inff();
        f32x16 o2 = {};
#pragma unroll 1
        for (int t = 0; t < 64; ++t) {
          const f32x16 z = {};
          f32x16 s = MFMA32(*(const LAS bf16x8*)(lds + LA_K + t * 1024 + lane * 16), __builtin_bit_cast(bf16x8, qf), z);
          float mt = s[0];
#pragma unroll
          for (int e = 1; e < 16; ++e) mt = fmaxf(mt, s[e]);
          auto rr = __builtin_amdgcn_permlane32_swap(__float_as_uint(mt), __float_as_uint(mt), false, false);
          const float mn = fmaxf(m, fmaxf(__uint_as_float(rr[0]), __uint_as_float(rr[1])));
          const float al = __builtin_amdgcn_exp2f(m - mn);
          m = mn;
          u32x4 pa, pb2;
#pragma unroll
          for (int e = 0; e < 4; ++e) {
            pa[e] = cvtpk(__builtin_amdgcn_exp2f(s[2 * e] - mn), __builtin_amdgcn_exp2f(s[2 * e + 1] - mn));
            pb2[e] = cvtpk(__builtin_amdgcn_exp2f(s[8 + 2 * e] - mn), __builtin_amdgcn_exp2f(s[9 + 2 * e] - mn));
          }
#pragma unroll
          for (int e = 0; e < 16; ++e) o2[e] *= al;
          const LAS char* vb = lds + LA_V + 2 * t * VBLK + h2 * 272 + (r < 16 ? r : 16) * 16;
          o2 = MFMA32(*(const LAS bf16x8*)(vb), __builtin_bit_cast(bf16x8, pa), o2);
          o2 = MFMA32(*(const LAS bf16x8*)(vb + VBLK), __builtin_bit_cast(bf16x8, pb2), o2);
        }
        o = o2;
      }
    }
    const float inv = 1.0f / o[8];
    u32x4 res = {cvtpk(o[0] * inv, o[1] * inv), cvtpk(o[2] * inv, o[3] * inv), cvtpk(o[4] * inv, o[5] * inv), cvtpk(o[6] * inv, o[7] * inv)};
#pragma unroll
    for (int e = 0; e < 4; ++e) if (res[e] == SENT) res[e] = 0x7fc07fc0u;
    st_sc1(rs, WS_OF + ((unsigned)(aT * 32 + ah) * 64 + lane) * 16, res);
  }
  {
    u32x4 of[4];
    if (w < 4) {
#pragma unroll
      for (int e = 22; e < 32; ++e) wf[e] = __builtin_bit_cast(bf16x8, ld_sc1(rs, WS_WOF + ((unsigned)((4 * cgp + w) * 32 + e) * 64 + lane) * 16));
      __builtin_amdgcn_s_sleep(TAIL_SLEEP);
    } else __builtin_amdgcn_s_sleep(TAIL_SLEEP2);
#pragma unroll
    for (int e = 0; e < 4; ++e) of[e] = ld_sc1(rs, WS_OF + ((unsigned)(T * 32 + 4 * w + e) * 64 + lane) * 16);
    if (w >= 4) {
      LAS char* tb = lds + LE_Y + wl * 1152;
      const int cg = lane & 7;
#pragma unroll
      for (int ps = 0; ps < 2; ++ps) {
        u32x4 o;
        unsigned yv[8];
#pragma unroll
        for (int hf = 0; hf < 2; ++hf) {
          *(LAS u32x4*)(tb + lane * 16 + (lane >> 3) * 16) = ey[hf];
          const LAS uint16_t* rp_ = (const LAS uint16_t*)(tb + cg * 144) + 2 * ((lane >> 3) & 3) + ps;
#pragma unroll
          for (int e = 0; e < 8; ++e) { const unsigned t = rp_[e * 8]; if (hf == 0 || (lane >> 5)) yv[e] = t; }
        }
        const int tl = 2 * (lane >> 3) + ps;
        const u32x4 kv = *(const LAS u32x4*)(lds + LA_Q + (16 * wl + tl) * 128 + cg * 16);
#pragma unroll
        for (int e = 0; e < 4; ++e) {
          const float ylo = __uint_as_float(yv[2 * e] << 16), yhi = __uint_as_float(yv[2 * e + 1] << 16);
          const unsigned t1 = cvtpk(bflo(kv[e]) * bflo(edd[e]), bfhi(kv[e]) * bfhi(edd[e]));
          const unsigned y2 = cvtpk(ylo + bflo(t1), yhi + bfhi(t1));
          o[e] = cvtpk(bflo(eq[ps][e]) * bflo(y2), bfhi(eq[ps][e]) * bfhi(y2));
        }
        __builtin_amdgcn_raw_buffer_store_b128(o, ro, ((unsigned)(64 * i + 16 * wl + tl) * 1024 + 64 * j + 8 * cg) * 2, 0, OUT_AUX);
      }
    }
    for (unsigned spins = 0; spins < SPIN_CAP; ++spins) {
      if (__all(max(max8(of[0], of[1]), max8(of[2], of[3])) != SENT)) break;
      __builtin_amdgcn_s_sleep(RETRY_SLEEP);
#pragma unroll
      for (int e = 0; e < 4; ++e) of[e] = ld_sc1(rs, WS_OF + ((unsigned)(T * 32 + 4 * w + e) * 64 + lane) * 16);
    }
    RAW_BAR();
#pragma unroll
    for (int e = 0; e < 4; ++e) *(LAS u32x4*)(lds + (4 * w + e) * 1024 + lane * 16) = of[e];
    RAW_BAR();
    if (b == 0 && tid == 511) __hip_atomic_store((GAS u64*)(P.ws + WS_EPOCH), epoch + 1, RLX_AGENT);
    if (w < 4) {
      f32x16 acc = {};
      bf16x8 fa[8], fb[8];
#pragma unroll
      for (int e = 0; e < 8; ++e) fa[e] = *(const LAS bf16x8*)(lds + e * 1024 + lane * 16);
      SBAR();
#pragma unroll
      for (int c = 0; c < 2; ++c) {
#pragma unroll
        for (int e = 0; e < 8; ++e) fb[e] = *(const LAS bf16x8*)(lds + (16 * c + 8 + e) * 1024 + lane * 16);
#pragma unroll
        for (int e = 0; e < 8; ++e) acc = MFMA32(wf[16 * c + e], fa[e], acc);
#pragma unroll
        for (int e = 0; e < 8; ++e) { __builtin_amdgcn_sched_group_barrier(0x8, 1, 0); __builtin_amdgcn_sched_group_barrier(0x100, 1, 0); }
        SBAR();
        if (c == 0) {
#pragma unroll
          for (int e = 0; e < 8; ++e) fa[e] = *(const LAS bf16x8*)(lds + (16 + e) * 1024 + lane * 16);
        }
#pragma unroll
        for (int e = 0; e < 8; ++e) acc = MFMA32(wf[16 * c + 8 + e], fb[e], acc);
#pragma unroll
        for (int e = 0; e < 8; ++e) { __builtin_amdgcn_sched_group_barrier(0x8, 1, 0); __builtin_amdgcn_sched_group_barrier(0x100, 1, 0); }
        SBAR();
      }
      unsigned d[8];
#pragma unroll
      for (int q4 = 0; q4 < 8; ++q4) d[q4] = cvtpk(acc[2 * q4], acc[2 * q4 + 1]);
      const unsigned op = ((unsigned)(32 * T + (lane & 31)) * 1024 + 512 + 32 * (4 * cgp + w) + 8 * h2) * 2;
#pragma unroll
      for (int pr = 0; pr < 2; ++pr) {
        auto s0 = __builtin_amdgcn_permlane32_swap(d[4 * pr], d[4 * pr + 2], false, false);
        auto s1 = __builtin_amdgcn_permlane32_swap(d[4 * pr + 1], d[4 * pr + 3], false, false);
        __builtin_amdgcn_raw_buffer_store_b128((u32x4){s0[0], s1[0], s0[1], s1[1]}, ro, op + 32 * pr, 0, OUT_AUX);
      }
    }
  }
}

extern "C" void kernel_launch(void* const* d_in, const int* in_sizes, int n_in, void* d_out, int out_size, void* d_ws, size_t ws_size, hipStream_t stream) {
  Params P;
  P.aq = (const uint16_t*)d_in[0]; P.ak = (const uint16_t*)d_in[1]; P.av = (const uint16_t*)d_in[2]; P.shiftk = (const uint16_t*)d_in[3];
  P.ssmk = (const uint16_t*)d_in[4]; P.ssmd = (const uint16_t*)d_in[5]; P.x = (const uint16_t*)d_in[6]; P.wqkv = (const uint16_t*)d_in[7]; P.wo = (const uint16_t*)d_in[8];
  P.out = (uint16_t*)d_out; P.ws = (char*)d_ws;
  k_fused<<<256, 512, 0, stream>>>(P);
}
```
